```python
import torch

def _softmax(x, axis=-1):
    return torch.softmax(x, dim=axis)
def _gelu(x):
    return torch.nn.functional.gelu(x, approximate="tanh")
def _silu(x):
    return torch.nn.functional.silu(x)
def _sigmoid(x):
    return torch.sigmoid(x)
def _layernorm(x, axis=-1, eps=1e-5):
    m = x.mean(dim=axis, keepdim=True)
    v = x.var(dim=axis, keepdim=True, unbiased=False)
    return (x - m) / torch.sqrt(v + eps)
def _scatter_add(tgt, idx, src, axis=0):
    return tgt.clone().index_add_(axis, idx, src)
def _segment_sum(src, seg, n):
    out = torch.zeros((n,) + src.shape[1:], dtype=src.dtype)
    return out.index_add_(0, seg, src)
def _pad(x, pw):
    flat = tuple(v for pair in reversed(pw) for v in pair)
    return torch.nn.functional.pad(x, flat)

kernel_name = 'interleave_layers__grouped_kv_mqa__dense_activation_mish__prompt'

def setup_inputs():

    _w = (lambda a: (lambda t: t.to(torch.bfloat16) if t.is_floating_point() else t)(torch.as_tensor(a)))
    def _A_setup():
        B, S, D, H = 1, 2048, 640, 32
        Dh = D // H
        return {
            "x": _w((lambda *s: torch.randn(*s, dtype=torch.bfloat16) * 0.05)(B, S, D)),
            "wq": _w((lambda *s: torch.randn(*s, dtype=torch.bfloat16) * 0.05)(D, H * Dh)),
            "wk": _w((lambda *s: torch.randn(*s, dtype=torch.bfloat16) * 0.05)(D, Dh)),
            "wv": _w((lambda *s: torch.randn(*s, dtype=torch.bfloat16) * 0.05)(D, Dh)),
            "wo": _w((lambda *s: torch.randn(*s, dtype=torch.bfloat16) * 0.05)(H * Dh, D)),
        }
    _A_in = _A_setup()

    def _B_setup():
        B, S, D, F = 1, 2048, 640, 1728
        return {
            "x": _w((lambda *s: torch.randn(*s, dtype=torch.bfloat16) * 0.05)(B, S, D)),
            "w1": _w((lambda *s: torch.randn(*s, dtype=torch.bfloat16) * 1.6)(D, F) * (D ** -0.5)),
            "b1": _w((lambda *s: torch.randn(*s, dtype=torch.bfloat16) * 0.2)(F)),
            "w2": _w((lambda *s: torch.randn(*s, dtype=torch.bfloat16) * 0.2)(F, D) * (F ** -0.5)),
        }
    _B_in = _B_setup()

    return {
        "A_x": _A_in["x"],
        "A_wq": _A_in["wq"],
        "A_wk": _A_in["wk"],
        "A_wv": _A_in["wv"],
        "A_wo": _A_in["wo"],
        "B_x": _B_in["x"],
        "B_w1": _B_in["w1"],
        "B_b1": _B_in["b1"],
        "B_w2": _B_in["w2"],
    }

def reference(A_x, A_wq, A_wk, A_wv, A_wo, B_x, B_w1, B_b1, B_w2):
    def _A_ref(x, wq, wk, wv, wo):
        B, S, D = x.shape
        H = 32
        Dh = D // H
        q = (lambda x, *d: x.permute(*d))((x @ wq).reshape(B, S, H, Dh), 0, 2, 1, 3)
        k = x @ wk
        v = x @ wv
        scores = torch.einsum("bhqd,bkd->bhqk", q, k) * (Dh ** -0.5)
        p = _softmax(scores, axis=-1)
        o = torch.einsum("bhqk,bkd->bhqd", p, v)
        return (lambda x, *d: x.permute(*d))(o, 0, 2, 1, 3).reshape(B, S, H * Dh) @ wo

    def _B_ref(x, w1, b1, w2):
        h = torch.einsum("bsd,df->bsf", x, w1) + b1
        sp = torch.log1p(torch.exp(-torch.abs(h))) + torch.where(h > 0, h, torch.zeros_like(h))
        act = h * torch.tanh(sp)
        return torch.einsum("bsf,fd->bsd", act, w2)

    x = A_x
    x = x + _A_ref(_layernorm(x, axis=-1), A_wq, A_wk, A_wv, A_wo)
    x = x + _B_ref(_layernorm(x, axis=-1), B_w1, B_b1, B_w2)
    x = x + _A_ref(_layernorm(x, axis=-1), A_wq, A_wk, A_wv, A_wo)
    x = x + _B_ref(_layernorm(x, axis=-1), B_w1, B_b1, B_w2)
    return x
```

```cpp
#include <hip/hip_runtime.h>
#include <hip/hip_bf16.h>
#include <stdint.h>

#define GAS __attribute__((address_space(1)))
#define LAS __attribute__((address_space(3)))
#define DI __device__ __forceinline__
typedef unsigned short u16;
typedef unsigned v4u __attribute__((ext_vector_type(4)));
typedef unsigned v2u __attribute__((ext_vector_type(2)));
typedef float f32x2 __attribute__((ext_vector_type(2)));
typedef float f32x4 __attribute__((ext_vector_type(4)));
typedef float f32x16 __attribute__((ext_vector_type(16)));
typedef short s16x4 __attribute__((ext_vector_type(4)));
typedef short s16x8 __attribute__((ext_vector_type(8)));
typedef __bf16 bf16x2 __attribute__((ext_vector_type(2)));
typedef GAS unsigned gu32;
#define RLX_AGENT __ATOMIC_RELAXED, __HIP_MEMORY_SCOPE_AGENT

constexpr int S = 2048, D = 640, F = 1728;
constexpr int NG = 32;
constexpr int NU = 54;
constexpr float LN_EPS = 1e-5f;
constexpr float QSCALE = 0.22360679774997896f * 1.4426950408889634f;
constexpr float LOG2E = 1.4426950408889634f;
constexpr float SAFE_BOUND = 64.f;
constexpr float SAFE_VMAX = 1.0e12f;

constexpr int WS_CTRL = 0;
constexpr int CNT_KV = 0, CNT_O = 1, CNT_X1 = 2, CNT_H = 3, CNT_X2 = 4;
constexpr int N_CNT = 10 * NG;
constexpr int W_TMO = N_CNT * 64;
constexpr int WS_WQ = 131072;
constexpr int WS_WO = WS_WQ + 819200;
constexpr int WS_W1 = WS_WO + 819200;
constexpr int WS_W2 = WS_W1 + NU * 40960;
constexpr int WS_WKV = WS_W2 + 8 * 5 * NU * 1024;
constexpr int WS_CSQ = WS_WKV + 81920;
constexpr int WS_CSKV = WS_CSQ + 4096;
constexpr int WS_CS1 = WS_CSKV + 1024;
constexpr int ACT_G = 81920;
constexpr int ACT_L = NG * ACT_G;
constexpr int WS_XF = WS_CS1 + 8192;
constexpr int WS_OF = WS_XF + ACT_L;
constexpr int WS_X1F = WS_OF + ACT_L;
constexpr int ST_G = 16 * 64 * 8;
constexpr int ST_L = NG * ST_G;
constexpr int WS_STX = WS_X1F + ACT_L;
constexpr int WS_STX1 = WS_STX + ST_L;
constexpr int HF_G = 4 * NU * 1024;
constexpr int HF_L = NG * HF_G;
constexpr int WS_HF = WS_STX1 + ST_L;
constexpr int KV_TILE = 3072;
constexpr int KV_L = 64 * KV_TILE;
constexpr int WS_KV = WS_HF + HF_L;
constexpr int WS_KVN = WS_KV + 2 * KV_L;
constexpr int WS_DBG = WS_KVN + 4096;
constexpr int WS_END = WS_DBG + 2 * 256 * 64 * 8 + 8192;

constexpr int L_ACT = 0;
constexpr int L_QS = 49152;
constexpr int QS_STRIDE = 176;
constexpr int L_XO = 60416;
constexpr int L_RED = 81920;
constexpr int L_STASH = 143360;
constexpr int L_REDKV = L_STASH + 10240;
constexpr int L_STAT = L_REDKV + 8192;
constexpr int LDS_BYTES = L_STAT + 512;

#define STAMPS 0
#define WARM_CODE 1
#define WARM_PREP 1
#define WARM_TLB 0

DI unsigned pack2(float a, float b) { bf16x2 v = {(__bf16)a, (__bf16)b}; return __builtin_bit_cast(unsigned, v); }
DI float u2f(unsigned u) { return __builtin_bit_cast(float, u); }
DI unsigned f2u(float f) { return __builtin_bit_cast(unsigned, f); }
DI float bflo(unsigned u) { return __builtin_bit_cast(float, u << 16); }
DI float bfhi(unsigned u) { return __builtin_bit_cast(float, u & 0xffff0000u); }
DI f32x4 mfma16(v4u a, v4u b, f32x4 c) {
    return __builtin_amdgcn_mfma_f32_16x16x32_bf16(__builtin_bit_cast(s16x8, a), __builtin_bit_cast(s16x8, b), c, 0, 0, 0);
}
DI f32x16 mfma32(v4u a, v4u b, f32x16 c) {
    return __builtin_amdgcn_mfma_f32_32x32x16_bf16(__builtin_bit_cast(s16x8, a), __builtin_bit_cast(s16x8, b), c, 0, 0, 0);
}
DI f32x16 mfma32k8(v2u a, v2u b, f32x16 c) {
    return __builtin_amdgcn_mfma_f32_32x32x8bf16_1k(__builtin_bit_cast(s16x4, a), __builtin_bit_cast(s16x4, b), c, 0, 0, 0);
}
#define SBAR() __builtin_amdgcn_sched_barrier(0)
#define BAR() asm volatile("s_waitcnt lgkmcnt(0)\n\ts_barrier" ::: "memory")
#define QBAR() asm volatile("s_barrier" ::: "memory")
#define DRAIN() do { __builtin_amdgcn_s_waitcnt(0x0F70); asm volatile("s_waitcnt vmcnt(0)" ::: "memory"); } while (0)
#define MEMFENCE() asm volatile("" ::: "memory")
template <int N> DI void wait_vm() {
    __builtin_amdgcn_s_waitcnt((N & 15) | ((N >> 4) << 14) | 0x0F70);
    asm volatile("s_waitcnt vmcnt(%0)" ::"i"(N) : "memory");
}
template <int N> DI void wait_vm_bar() {
    __builtin_amdgcn_s_waitcnt((N & 15) | ((N >> 4) << 14) | 0x0F70);
    asm volatile("s_waitcnt vmcnt(%0) lgkmcnt(0)\n\ts_barrier" ::"i"(N) : "memory");
}
template <int OFF> DI void dma(unsigned voff, unsigned long long sbase, unsigned ldsdst) {
    asm volatile("s_mov_b32 m0, %2\n\ts_nop 0\n\tglobal_load_lds_dwordx4 %0, %1 offset:%3 sc1" ::"v"(voff), "s"(sbase), "s"(ldsdst), "i"(OFF) : "memory");
}
typedef __amdgpu_buffer_rsrc_t rsrc_t;
constexpr int SC1 = 16;
DI void st128(rsrc_t rs, v4u v, int off) { __builtin_amdgcn_raw_buffer_store_b128(v, rs, off, 0, SC1); }
DI v4u ldw(rsrc_t rs, unsigned lane16, int soff) { return __builtin_amdgcn_raw_buffer_load_b128(rs, lane16, soff, 0); }
template <typename T> DI T lds_ld(LAS unsigned char* L, int off) { return *(LAS const T*)(L + off); }
template <typename T> DI void lds_st(LAS unsigned char* L, int off, T v) { *(LAS T*)(L + off) = v; }
DI float sum16(float v) { const auto r = __builtin_amdgcn_permlane16_swap(f2u(v), f2u(v), false, false); return u2f(r[0]) + u2f(r[1]); }
DI float sum32(float v) { const auto r = __builtin_amdgcn_permlane32_swap(f2u(v), f2u(v), false, false); return u2f(r[0]) + u2f(r[1]); }
DI float max32(float v) { const auto r = __builtin_amdgcn_permlane32_swap(f2u(v), f2u(v), false, false); return fmaxf(u2f(r[0]), u2f(r[1])); }

constexpr unsigned SPIN_CAP = 1u << 17;
DI void wait_cnt(gu32* ctrl, int idx, unsigned want, int tid) {
    if (tid == 0) {
        unsigned spins = SPIN_CAP;
#pragma unroll 1
        while (__hip_atomic_load(ctrl + idx * 64, RLX_AGENT) < want && --spins) {}
        if (spins == 0) __hip_atomic_store(ctrl + W_TMO, 1u, RLX_AGENT);
        __builtin_amdgcn_fence(__ATOMIC_ACQUIRE, "wavefront");
    }
    BAR();
}
DI void signal_cnt(gu32* ctrl, int idx, int tid) {
    DRAIN();
    BAR();
    if (tid == 0) __hip_atomic_fetch_add(ctrl + idx * 64, 1u, RLX_AGENT);
}
DI int cnt_idx(int kind, int layer, int g) { return (kind * 2 + layer) * NG + g; }

#define STAMPW(i) do { } while (0)
#define STAMP(i) do { } while (0)

extern "C" __global__ void main_kernel(const u16* __restrict__, u16* __restrict__, unsigned char* __restrict__);
extern "C" __global__ void end_marker_kernel();
constexpr int PB_PACK = 185, PB_KV = 20, PB_CS = 37, PB_CSKV = 2, PB_X = 128;
constexpr int PB_TOTAL = PB_PACK + PB_KV + PB_CS + PB_CSKV + PB_X + 1;

DI void transpose_store(const v4u (&r)[8], unsigned char* ws, int dst, LAS unsigned char* img) {
    const int lane = threadIdx.x & 63;
#pragma unroll
    for (int f = 0; f < 8; ++f) {
        v4u o;
#pragma unroll
        for (int m = 0; m < 4; ++m)
            o[m] = __builtin_amdgcn_perm(r[2 * m + 1][f >> 1], r[2 * m][f >> 1], (f & 1) ? 0x07060302u : 0x05040100u);
        *(LAS v4u*)(img + lane * 128 + ((f ^ (lane & 7)) * 16)) = o;
    }
    *(LAS int*)(img + 8192 + lane * 4) = dst;
    const int f = lane & 7;
#pragma unroll
    for (int i = 0; i < 8; ++i) {
        const int item = i * 8 + (lane >> 3);
        const int d = *(LAS const int*)(img + 8192 + item * 4);
        *(GAS v4u*)(ws + d + 16 * f) = *(LAS const v4u*)(img + item * 128 + ((f ^ (item & 7)) * 16));
    }
}

__global__ __launch_bounds__(256) void prep_kernel(const u16* __restrict__ x, const u16* __restrict__ wq, const u16* __restrict__ wk,
                                                   const u16* __restrict__ wv, const u16* __restrict__ wo, const u16* __restrict__ w1,
                                                   const u16* __restrict__ w2, unsigned char* __restrict__ ws) {
    __shared__ __attribute__((aligned(16))) float red[32 * 64 + 1024 + 4 * 2112];
    const int tid = threadIdx.x;
    int b = blockIdx.x;
    {
        unsigned lo0, hi0, lo1, hi1;
        asm volatile("s_getpc_b64 s[40:41]\n\ts_add_u32 s40, s40, main_kernel@rel32@lo+4\n\ts_addc_u32 s41, s41, main_kernel@rel32@hi+12\n\t"
                     "s_mov_b32 %0, s40\n\ts_mov_b32 %1, s41\n\t"
                     "s_getpc_b64 s[40:41]\n\ts_add_u32 s40, s40, end_marker_kernel@rel32@lo+4\n\ts_addc_u32 s41, s41, end_marker_kernel@rel32@hi+12\n\t"
                     "s_mov_b32 %2, s40\n\ts_mov_b32 %3, s41"
                     : "=s"(lo0), "=s"(hi0), "=s"(lo1), "=s"(hi1) :: "s40", "s41", "scc");
        const unsigned long long c0 = ((unsigned long long)hi0 << 32) | lo0, c1 = ((unsigned long long)hi1 << 32) | lo1;
        const unsigned off = ((unsigned)(b >> 3) * 256u + tid) * 16u;
        if (c0 + off + 16 <= c1)
            asm volatile("s_mov_b32 m0, %2\n\ts_nop 0\n\tglobal_load_lds_dwordx4 %0, %1" ::"v"(off), "s"(c0), "s"(8192u + (unsigned)__builtin_amdgcn_readfirstlane(tid >> 6) * 1024u) : "memory");
    }
    if (b < PB_CS) {
        const bool isq = b < 10;
        const int col0 = (isq ? b : b - 10) * 64 + (tid & 7) * 8, sl = tid >> 3, ld = isq ? D : F;
        const u16* w = (isq ? wq : w1) + (size_t)(sl * 20) * ld + col0;
        float a[8] = {0.f, 0.f, 0.f, 0.f, 0.f, 0.f, 0.f, 0.f};
        v4u v[20];
#pragma unroll
        for (int i = 0; i < 20; ++i) v[i] = *(const v4u*)(w + (size_t)i * ld);
#pragma unroll
        for (int i = 0; i < 20; ++i)
#pragma unroll
            for (int m = 0; m < 4; ++m) { a[2 * m] += bflo(v[i][m]); a[2 * m + 1] += bfhi(v[i][m]); }
#pragma unroll
        for (int m = 0; m < 8; ++m) red[sl * 64 + (tid & 7) * 8 + m] = a[m];
        __syncthreads();
        if (tid < 64) {
            float s = 0.f;
#pragma unroll
            for (int i = 0; i < 32; ++i) s += red[i * 64 + tid];
            ((float*)(ws + (isq ? WS_CSQ : WS_CS1)))[(isq ? b : b - 10) * 64 + tid] = s;
        }
        return;
    }
    b -= PB_CS;
    if (b < PB_CSKV) {
        const unsigned* w = (const unsigned*)(b ? wv : wk);
        if (tid < 160) {
            unsigned v[40];
#pragma unroll
            for (int i = 0; i < 40; ++i) v[i] = w[tid + 160 * i];
            float a0 = 0.f, a1 = 0.f;
#pragma unroll
            for (int i = 0; i < 40; ++i) { a0 += bflo(v[i]); a1 += bfhi(v[i]); }
            red[tid * 2] = a0; red[tid * 2 + 1] = a1;
        }
        __syncthreads();
        if (tid < 32) {
            float s = 0.f;
            if (tid < 20) {
#pragma unroll
                for (int i = 0; i < 16; ++i) s += red[i * 20 + tid];
            }
            ((float*)(ws + WS_CSKV))[b * 32 + tid] = s;
        }
        return;
    }
    b -= PB_CSKV;
    if (b < PB_PACK) {
        LAS unsigned char* img = (LAS unsigned char*)red + 12288 + (tid >> 6) * 8448;
        int it = b * 256 + tid;
        v4u r[8];
        if (it < 12800) {
            const bool isq = it < 6400;
            if (!isq) it -= 6400;
            const int ko = it / 80, ch = it % 80, col0 = 8 * ch;
            const int c = col0 / 80, ft = (col0 % 80) >> 4;
            const int fh2 = ((col0 % 80) & 15) >> 3;
            if (isq) {
#pragma unroll
                for (int j = 0; j < 8; ++j) r[j] = *(const v4u*)(wq + (size_t)(8 * ko + j) * D + col0);
            } else {
                const int cp = ko / 10, cc = ko % 10;
#pragma unroll
                for (int j = 0; j < 8; ++j) {
                    const int k = cc < 8 ? 80 * cp + 20 * (cc >> 1) + 4 * (cc & 1) + (j & 3) + 8 * (j >> 2)
                                         : 80 * cp + 20 * (2 * (cc - 8) + (j >> 2)) + 16 + (j & 3);
                    r[j] = *(const v4u*)(wo + (size_t)k * D + col0);
                }
            }
            transpose_store(r, ws, (isq ? WS_WQ : WS_WO) + ((c * 5 + ft) * 20 + (ko >> 2)) * 1024 + ((ko & 3) * 16 + fh2 * 8) * 16, img);
        } else if (it < 12800 + 17280) {
            it -= 12800;
            const int ko = it / 216, ch = it % 216, col0 = 8 * ch;
#pragma unroll
            for (int j = 0; j < 8; ++j) r[j] = *(const v4u*)(w1 + (size_t)(8 * ko + j) * F + col0);
            const int u = col0 >> 5, ft = (col0 >> 4) & 1, fh = ch & 1;
            transpose_store(r, ws, WS_W1 + ((u * 2 + ft) * 20 + (ko >> 2)) * 1024 + ((ko & 3) * 16 + fh * 8) * 16, img);
        } else {
            it -= 12800 + 17280;
            const int ko = it / 80, ch = it % 80, col0 = 8 * ch;
            const int u = ko >> 2, q4 = ko & 3;
#pragma unroll
            for (int j = 0; j < 8; ++j) r[j] = *(const v4u*)(w2 + (size_t)(32 * u + 16 * (j >> 2) + 4 * q4 + (j & 3)) * D + col0);
            const int c = col0 / 80, ft = (col0 % 80) >> 4, fh = ((col0 % 80) & 15) >> 3;
            transpose_store(r, ws, WS_W2 + ((c * 5 + ft) * NU + u) * 1024 + (q4 * 16 + fh * 8) * 16, img);
        }
        return;
    }
    b -= PB_PACK;
    if (b < PB_X) {
        const int tok = tid & 15, p = tid >> 4, g = b >> 2, T = b & 3;
        const u16* row = x + (size_t)(b * 16 + tok) * D;
        float s = 0.f, q = 0.f;
        v4u v[5];
#pragma unroll
        for (int i = 0; i < 5; ++i) v[i] = *(const v4u*)(row + (p + 16 * i) * 8);
#pragma unroll
        for (int i = 0; i < 5; ++i) {
            const int ch = p + 16 * i;
            *(v4u*)(ws + WS_XF + g * ACT_G + (T * 20 + (ch >> 2)) * 1024 + ((ch & 3) * 16 + tok) * 16) = v[i];
#pragma unroll
            for (int m = 0; m < 4; ++m) { const float a = bflo(v[i][m]), c = bfhi(v[i][m]); s += a + c; q += a * a + c * c; }
        }
        s = sum32(sum16(s)); q = sum32(sum16(q));
        const int wv = tid >> 6, q4 = p & 3;
        *(f32x2*)(ws + WS_STX + g * ST_G + ((q4 == 0 ? wv : 3 + wv * 3 + q4) * 64 + T * 16 + tok) * 8) = q4 == 0 ? (f32x2){s, q} : (f32x2){0.f, 0.f};
        return;
    }
    b -= PB_X;
    if (b < PB_KV) {
        const int it = b * 256 + tid, l = it & 63, fr = it >> 6;
        const int ks = fr % 20, ft = (fr / 20) & 1, kind = fr / 40;
        const int f = 16 * ft + (l & 15), k0 = 32 * ks + 8 * (l >> 4);
        const u16* w = kind ? wv : wk;
        v4u o = {0u, 0u, 0u, 0u};
        if (f < 20) {
#pragma unroll
            for (int m = 0; m < 4; ++m) o[m] = (unsigned)w[(k0 + 2 * m) * 20 + f] | ((unsigned)w[(k0 + 2 * m + 1) * 20 + f] << 16);
        }
        *(v4u*)(ws + WS_WKV + fr * 1024 + l * 16) = o;
        return;
    }
    for (int i = tid; i <= N_CNT; i += 256) ((unsigned*)(ws + WS_CTRL))[i * 64] = 0u;
}

struct Ctx {
    unsigned char* ws; rsrc_t rs; LAS unsigned char* L; gu32* ctrl;
    int tid, lane, w, g, c; unsigned lane16;
};

DI Ctx local_ctx(const Ctx& k0) {
    Ctx k = k0;
    asm volatile("" : "+s"(k.w), "+s"(k.g), "+s"(k.c));
    asm volatile("v_mbcnt_lo_u32_b32 %0, -1, 0\n\tv_mbcnt_hi_u32_b32 %0, -1, %0" : "=v"(k.lane));
    k.tid = k.w * 64 + k.lane; k.lane16 = k.lane * 16;
    return k;
}
DI unsigned long long gaddr(const Ctx& k, int off) { return (unsigned long long)k.ws + (unsigned)off; }

DI void gld128(v4u& d, unsigned voff, unsigned long long sbase) { asm volatile("global_load_dwordx4 %0, %1, %2" : "=v"(d) : "v"(voff), "s"(sbase) : "memory"); }
DI void gld128f(f32x4& d, unsigned voff, unsigned long long sbase) { asm volatile("global_load_dwordx4 %0, %1, %2" : "=v"(d) : "v"(voff), "s"(sbase) : "memory"); }
DI void gld32f(float& d, unsigned voff, unsigned long long sbase) { asm volatile("global_load_dword %0, %1, %2" : "=v"(d) : "v"(voff), "s"(sbase) : "memory"); }
#define PINV(x) asm volatile("" : "+v"(x))

struct Stats { v2u p[16]; };
DI void stats_issue(const Ctx& k, int base, Stats& st) {
    const unsigned long long sb = gaddr(k, base + k.g * ST_G);
    const unsigned vo = k.lane * 8;
#define ST_LD(i) asm volatile("global_load_dwordx2 %0, %1, %2 offset:%3 sc1" : "=v"(st.p[i]) : "v"(vo), "s"(sb + ((i) >> 3) * 4096), "i"(((i) & 7) * 512) : "memory")
    ST_LD(0); ST_LD(1); ST_LD(2); ST_LD(3); ST_LD(4); ST_LD(5); ST_LD(6); ST_LD(7);
    ST_LD(8); ST_LD(9); ST_LD(10); ST_LD(11); ST_LD(12); ST_LD(13); ST_LD(14); ST_LD(15);
#undef ST_LD
}
DI void stats_finish(const Ctx& k, Stats& st) {
    asm volatile("" : "+v"(st.p[0]), "+v"(st.p[1]), "+v"(st.p[2]), "+v"(st.p[3]), "+v"(st.p[4]), "+v"(st.p[5]), "+v"(st.p[6]), "+v"(st.p[7]));
    asm volatile("" : "+v"(st.p[8]), "+v"(st.p[9]), "+v"(st.p[10]), "+v"(st.p[11]), "+v"(st.p[12]), "+v"(st.p[13]), "+v"(st.p[14]), "+v"(st.p[15]));
    float s = 0.f, q = 0.f;
#pragma unroll
    for (int i = 0; i < 16; ++i) { s += u2f(st.p[i].x); q += u2f(st.p[i].y); }
    const float mean = s * (1.f / D), var = fmaxf(q * (1.f / D) - mean * mean, 0.f);
    lds_st<f32x2>(k.L, L_STAT + k.lane * 8, (f32x2){mean, __builtin_amdgcn_rsqf(var + LN_EPS)});
}

template <int NFT> DI void gemm80_load(const Ctx& k, int wbase, v4u (&wf)[15]) {
    const int kq = k.w & 3, ft0 = NFT == 3 ? 0 : 3;
#pragma unroll
    for (int i = 0; i < 5; ++i)
#pragma unroll
        for (int a = 0; a < NFT; ++a) wf[a * 5 + i] = ldw(k.rs, k.lane16, wbase + ((k.c * 5 + ft0 + a) * 20 + 5 * kq + i) * 1024);
    MEMFENCE(); SBAR();
}
template <int NFT> DI v4u gemm80_w(const Ctx& k, int wbase, int j) {
    return ldw(k.rs, k.lane16, wbase + ((k.c * 5 + (NFT == 3 ? 0 : 3) + j / 5) * 20 + 5 * (k.w & 3) + j % 5) * 1024);
}
DI void gemm80_dma(const Ctx& k, int base) {
    const int gb = base + k.g * ACT_G, kq = k.w & 3;
#pragma unroll
    for (int st = 0; st < 2; ++st) {
        const int o = (((kq + 1 + (k.w >> 2) + 2 * st) & 3) * 20 + 5 * kq) * 1024;
        const unsigned long long sb = gaddr(k, gb + o);
        dma<0>(k.lane16, sb, L_ACT + o); dma<1024>(k.lane16, sb, L_ACT + o); dma<2048>(k.lane16, sb, L_ACT + o); dma<3072>(k.lane16, sb, L_ACT + o);
        dma<0>(k.lane16, sb + 4096, L_ACT + o + 4096);
        if (st == 0) QBAR();
    }
}
template <int NFT, bool STAGED, class Hook> DI void gemm80_mma(const Ctx& k, const v4u (&wf)[15], f32x4 (&acc)[3], Hook hook) {
    const int kq = k.w & 3, ft0 = NFT == 3 ? 0 : 3;
    int ab[4];
#pragma unroll
    for (int j = 0; j < 4; ++j) ab[j] = L_ACT + (((kq + 1 + j) & 3) * 20 + 5 * kq) * 1024 + k.lane16;
    v4u b[4];
#pragma unroll
    for (int n = 0; n < 3; ++n) b[n] = lds_ld<v4u>(k.L, ab[0] + n * 1024);
#pragma unroll
    for (int n = 0; n < 20; ++n) {
        if (STAGED && n == 7) wait_vm_bar<0>();
        hook(n);
        if (n + 3 < 20) b[(n + 3) & 3] = lds_ld<v4u>(k.L, ab[(n + 3) / 5] + ((n + 3) % 5) * 1024);
        if (n % 5 == 0) {
#pragma unroll
            for (int a = 0; a < NFT; ++a) acc[a] = (f32x4){0.f, 0.f, 0.f, 0.f};
        }
        SBAR();
#pragma unroll
        for (int a = 0; a < NFT; ++a) acc[a] = mfma16(wf[a * 5 + n % 5], b[n & 3], acc[a]);
        SBAR();
        if (n % 5 == 4 && n < 19) {
            const int j = n / 5;
#pragma unroll
            for (int a = 0; a < NFT; ++a) lds_st<f32x4>(k.L, L_RED + ((((kq + 1 + j) & 3) * 3 + j) * 5 + ft0 + a) * 1024 + k.lane16, acc[a]);
        }
    }
}
template <int NFT> DI void red_send(const Ctx& k, const f32x4 (&acc)[3][4]) {
    const int kq = k.w & 3, ft0 = NFT == 3 ? 0 : 3;
#pragma unroll
    for (int dT = 1; dT < 4; ++dT)
#pragma unroll
        for (int a = 0; a < NFT; ++a)
            lds_st<f32x4>(k.L, L_RED + ((((kq + dT) & 3) * 3 + dT - 1) * 5 + ft0 + a) * 1024 + k.lane16, acc[a][dT]);
}
template <int NFT> DI void red_recv(const Ctx& k, const f32x4 (&own)[3], f32x4 (&y)[3]) {
    const int kq = k.w & 3, ft0 = NFT == 3 ? 0 : 3;
    const int o = L_RED + (kq * 15 + ft0) * 1024 + k.lane16;
#pragma unroll
    for (int a = 0; a < NFT; ++a)
        y[a] = (own[a] + lds_ld<f32x4>(k.L, o + a * 1024)) + (lds_ld<f32x4>(k.L, o + (5 + a) * 1024) + lds_ld<f32x4>(k.L, o + (10 + a) * 1024));
}
DI int act_piece(const Ctx& k, int T, int ft) {
    const int q4 = k.lane >> 4, ch = 10 * k.c + 2 * ft + (q4 >> 1);
    return (T * 20 + (ch >> 2)) * 1024 + ((ch & 3) * 16 + (k.lane & 15)) * 16 + (q4 & 1) * 8;
}
template <int NFT, bool LAST> DI void resid_epilogue(const Ctx& k, const f32x4 (&y)[3], int actbase, int stbase, u16* out) {
    const int T = k.w & 3, ft0 = NFT == 3 ? 0 : 3;
    float s = 0.f, q = 0.f;
#pragma unroll
    for (int a = 0; a < NFT; ++a) {
        const int ft = ft0 + a;
        const v2u r = lds_ld<v2u>(k.L, L_STASH + (T * 5 + ft) * 512 + k.lane * 8);
        v2u o;
        o.x = pack2(bflo(r.x) + y[a][0], bfhi(r.x) + y[a][1]);
        o.y = pack2(bflo(r.y) + y[a][2], bfhi(r.y) + y[a][3]);
        if (LAST) {
            *(GAS v2u*)(out + (size_t)(k.g * 64 + T * 16 + (k.lane & 15)) * D + 80 * k.c + 16 * ft + 4 * (k.lane >> 4)) = o;
        } else {
            __builtin_amdgcn_raw_buffer_store_b64(o, k.rs, act_piece(k, T, ft), actbase + k.g * ACT_G, SC1);
            lds_st<v2u>(k.L, L_STASH + (T * 5 + ft) * 512 + k.lane * 8, o);
            const float a0 = bflo(o.x), a1 = bfhi(o.x), a2 = bflo(o.y), a3 = bfhi(o.y);
            s += (a0 + a1) + (a2 + a3);
            q += (a0 * a0 + a1 * a1) + (a2 * a2 + a3 * a3);
        }
    }
    if (!LAST) {
        s = sum32(sum16(s)); q = sum32(sum16(q));
        if (k.lane < 16) {
            v2u o = {f2u(s), f2u(q)};
            __builtin_amdgcn_raw_buffer_store_b64(o, k.rs, ((2 * k.c + (NFT == 3 ? 0 : 1)) * 64 + T * 16 + k.lane) * 8, stbase + k.g * ST_G, SC1);
        }
    }
}
DI float wave_max(float v) {
#define DPP_MAX(ctrl, rm) v = fmaxf(v, u2f(__builtin_amdgcn_update_dpp(0u, f2u(v), ctrl, rm, 0xf, true)))
    DPP_MAX(0x111, 0xf); DPP_MAX(0x112, 0xf); DPP_MAX(0x114, 0xf); DPP_MAX(0x118, 0xf);
    DPP_MAX(0x142, 0xa); DPP_MAX(0x143, 0xc);
#undef DPP_MAX
    return u2f(__builtin_amdgcn_readlane(f2u(v), 63));
}

DI v4u wkv_w(const Ctx& k, int i) { return ldw(k.rs, k.lane16, WS_WKV + (((k.c >> 2) * 2 + (k.w >> 2)) * 20 + 5 * (k.w & 3) + i) * 1024); }
DI void wkv_load(const Ctx& k, v4u (&wkv)[5]) {
    const int kq = k.w & 3, ftk = k.w >> 2, kind = k.c >> 2;
#pragma unroll
    for (int i = 0; i < 5; ++i) wkv[i] = ldw(k.rs, k.lane16, WS_WKV + ((kind * 2 + ftk) * 20 + 5 * kq + i) * 1024);
    MEMFENCE(); SBAR();
}
template <int NFT, int layer> DI void qkv_body(const Ctx& k0) {
    const Ctx k = local_ctx(k0);
    v4u wkv[5], wfe[15];
    const int kq = k.w & 3, ftk = k.w >> 2, kind = k.c >> 2, Tk = k.c & 3;
    const int q4 = k.lane >> 4, l15 = k.lane & 15, ft0 = NFT == 3 ? 0 : 3;
    const bool statw = NFT == 2 && k.w == 7;
    f32x4 csq[NFT], csk[2]; float csv[2];
    Stats st;
    const int gb = WS_XF + k.g * ACT_G;
    auto dma_first = [&] {
        const int o = (Tk * 20 + k.w) * 1024;
        dma<0>(k.lane16, gaddr(k, gb + o), L_ACT + o);
        dma<0>(k.lane16, gaddr(k, gb + o + 8192), L_ACT + o + 8192);
        if (k.w < 4) dma<0>(k.lane16, gaddr(k, gb + o + 16384), L_ACT + o + 16384);
    };
    auto dma_rest = [&] {
#pragma unroll
        for (int m = 0; m < 8; ++m) {
            const int j = k.w + 8 * m;
            if (m < 7 || k.w < 4) {
                int f = (Tk + 1) * 20 + j;
                f = f >= 80 ? f - 80 : f;
                dma<0>(k.lane16, gaddr(k, gb + f * 1024), L_ACT + f * 1024);
            }
        }
    };
    auto kv_mma = [&] {
        f32x4 a = {0.f, 0.f, 0.f, 0.f};
        const int base = L_ACT + (Tk * 20 + 5 * kq) * 1024 + k.lane16;
        v4u b[5];
#pragma unroll
        for (int i = 0; i < 5; ++i) b[i] = lds_ld<v4u>(k.L, base + i * 1024);
        if (kind == 0) {
#pragma unroll
            for (int i = 0; i < 5; ++i) a = mfma16(wkv[i], b[i], a);
        } else {
#pragma unroll
            for (int i = 0; i < 5; ++i) a = mfma16(b[i], wkv[i], a);
        }
        lds_st<f32x4>(k.L, L_REDKV + (kq * 2 + ftk) * 1024 + k.lane16, a);
    };
    if (layer == 0) {
        if (statw) stats_issue(k, WS_STX, st);
#pragma unroll
        for (int a = 0; a < NFT; ++a) gld128f(csq[a], q4 * 16, gaddr(k, WS_CSQ + (80 * k.c + 16 * (ft0 + a)) * 4));
        if (NFT == 2) {
#pragma unroll
            for (int ft = 0; ft < 2; ++ft) { gld128f(csk[ft], q4 * 16, gaddr(k, WS_CSKV + 64 * ft)); gld32f(csv[ft], l15 * 4, gaddr(k, WS_CSKV + (32 + 16 * ft) * 4)); }
        }
#pragma unroll
        for (int i = 0; i < 5; ++i) gld128(wkv[i], k.lane16, gaddr(k, WS_WKV + ((kind * 2 + ftk) * 20 + 5 * kq + i) * 1024));
        if (statw) gemm80_load<NFT>(k, WS_WQ, wfe);
        dma_first(); QBAR(); dma_rest();
        wait_vm_bar<7>();
#pragma unroll
        for (int a = 0; a < NFT; ++a) PINV(csq[a]);
        if (NFT == 2) { PINV(csk[0]); PINV(csk[1]); PINV(csv[0]); PINV(csv[1]); }
#pragma unroll
        for (int i = 0; i < 5; ++i) PINV(wkv[i]);
        STAMP(2);
        kv_mma();
    } else {
#pragma unroll
        for (int a = 0; a < NFT; ++a) csq[a] = *(const GAS f32x4*)(k.ws + WS_CSQ + (80 * k.c + 16 * (ft0 + a) + 4 * q4) * 4);
        if (NFT == 2) {
#pragma unroll
            for (int ft = 0; ft < 2; ++ft) {
                csk[ft] = *(const GAS f32x4*)(k.ws + WS_CSKV + (16 * ft + 4 * q4) * 4);
                csv[ft] = *(const GAS float*)(k.ws + WS_CSKV + (32 + 16 * ft + l15) * 4);
            }
        }
        wkv_load(k, wkv);
        if (statw) gemm80_load<NFT>(k, WS_WQ, wfe);
        wait_cnt(k.ctrl, cnt_idx(CNT_X2, 0, k.g), 8u, k.tid);
        wait_vm<0>();
        STAMP(25);
        if (statw) stats_issue(k, WS_STX, st);
        dma_first(); QBAR(); dma_rest();
        wait_vm_bar<7>();
        STAMP(26);
        kv_mma();
    }
    if (statw) stats_finish(k, st);
    BAR();
    auto stash_x = [&] {
        if (layer == 0) {
#pragma unroll
            for (int a = 0; a < NFT; ++a)
                lds_st<v2u>(k.L, L_STASH + (kq * 5 + ft0 + a) * 512 + k.lane * 8, lds_ld<v2u>(k.L, L_ACT + act_piece(k, kq, ft0 + a)));
        }
    };
    f32x4 acc[3];
    if (statw) {
        v2u kvo[2];
        float nrm = 0.f;
#pragma unroll
        for (int ft = 0; ft < 2; ++ft) {
            const int o = L_REDKV + ft * 1024 + k.lane16;
            const f32x4 a = (lds_ld<f32x4>(k.L, o) + lds_ld<f32x4>(k.L, o + 2048)) + (lds_ld<f32x4>(k.L, o + 4096) + lds_ld<f32x4>(k.L, o + 6144));
            if (kind == 0) {
                const f32x2 sv = lds_ld<f32x2>(k.L, L_STAT + (Tk * 16 + l15) * 8);
                const f32x4 cv = csk[ft];
                v2u o2;
                o2.x = pack2(sv.y * (a[0] - sv.x * cv[0]), sv.y * (a[1] - sv.x * cv[1]));
                o2.y = pack2(sv.y * (a[2] - sv.x * cv[2]), sv.y * (a[3] - sv.x * cv[3]));
                const float n2 = bflo(o2.x) * bflo(o2.x) + bfhi(o2.x) * bfhi(o2.x) + bflo(o2.y) * bflo(o2.y) + bfhi(o2.y) * bfhi(o2.y);
                if (ft == 0 || q4 == 0) nrm += n2;
                kvo[ft] = o2;
            } else {
                const int d = 16 * ft + l15;
                const float cv = csv[ft];
                float v[4];
#pragma unroll
                for (int r = 0; r < 4; ++r) {
                    const f32x2 sv = lds_ld<f32x2>(k.L, L_STAT + (Tk * 16 + 4 * q4 + r) * 8);
                    v[r] = d < 20 ? sv.y * (a[r] - sv.x * cv) : (d == 20 ? 1.f : 0.f);
                }
                const v2u o2 = {pack2(v[0], v[1]), pack2(v[2], v[3])};
                if (d < 20) nrm = fmaxf(fmaxf(nrm, fmaxf(fabsf(bflo(o2.x)), fabsf(bfhi(o2.x)))), fmaxf(fabsf(bflo(o2.y)), fabsf(bfhi(o2.y))));
                if (!(v[0] == v[0] && v[1] == v[1] && v[2] == v[2] && v[3] == v[3])) nrm = __builtin_inff();
                kvo[ft] = o2;
            }
        }
        if (kind == 0) { nrm = sum32(sum16(nrm)); if (!(nrm == nrm)) nrm = __builtin_inff(); }
        nrm = wave_max(nrm);
        wait_vm_bar<0>();
        const int tile = WS_KV + layer * KV_L + (k.g * 2 + (Tk >> 1)) * KV_TILE;
        if (kind == 0) {
            const int kk = 16 * (Tk & 1) + l15;
            __builtin_amdgcn_raw_buffer_store_b64(kvo[0], k.rs, ((q4 >> 1) * 32 + kk) * 16 + (q4 & 1) * 8, tile, SC1);
            if (q4 == 0) __builtin_amdgcn_raw_buffer_store_b64(kvo[1], k.rs, 1024 + kk * 8, tile, SC1);
        } else {
            const int o = 1280 + (Tk & 1) * 768 + ((q4 & 1) * 24 + l15) * 16 + (q4 >> 1) * 8;
            __builtin_amdgcn_raw_buffer_store_b64(kvo[0], k.rs, o, tile, SC1);
            if (l15 < 8) __builtin_amdgcn_raw_buffer_store_b64(kvo[1], k.rs, o + 256, tile, SC1);
        }
        if (k.lane == 0) __builtin_amdgcn_raw_buffer_store_b32(f2u(nrm), k.rs, (kind * 128 + k.g * 4 + Tk) * 4, WS_KVN + layer * 1024, SC1);
        QBAR();
        DRAIN();
        if (k.lane == 0) __hip_atomic_fetch_add(k.ctrl + cnt_idx(CNT_KV, layer, k.g) * 64, 1u, RLX_AGENT);
        STAMPW(layer == 0 ? 1 : 24);
        stash_x();
        gemm80_mma<NFT, false>(k, wfe, acc, [](int) {});
    } else {
        v4u wf[15];
        wait_vm_bar<0>();
        QBAR();
        gemm80_load<NFT>(k, WS_WQ, wf);
        stash_x();
        gemm80_mma<NFT, false>(k, wf, acc, [](int) {});
    }
    STAMP(3 + layer * 24);
    BAR();
    STAMP(4 + layer * 24);
    {
        f32x4 y[3];
        red_recv<NFT>(k, acc, y);
        const f32x2 sv = lds_ld<f32x2>(k.L, L_STAT + (kq * 16 + l15) * 8);
        const float r = sv.y * QSCALE;
#pragma unroll
        for (int a = 0; a < NFT; ++a) {
            const f32x4 cv = csq[a];
            v2u o = {pack2(r * (y[a][0] - sv.x * cv[0]), r * (y[a][1] - sv.x * cv[1])), pack2(r * (y[a][2] - sv.x * cv[2]), r * (y[a][3] - sv.x * cv[3]))};
            lds_st<v2u>(k.L, L_QS + (kq * 16 + l15) * QS_STRIDE + (16 * (ft0 + a) + 4 * q4) * 2, o);
        }
    }
    BAR();
}

constexpr int KV_SLOT = 24576;
template <int I> DI void kv_piece(const Ctx& k, int layer, int ci) {
    const int half = k.w >> 2, p0 = (k.w & 3) * 3;
    dma<I * 1024>(k.lane16, gaddr(k, WS_KV + layer * KV_L + (half * 32 + ci * 4) * KV_TILE + p0 * 1024), L_ACT + (ci & 1) * KV_SLOT + k.w * 3072);
}
DI void kv_dma(const Ctx& k, int layer, int ci) { kv_piece<0>(k, layer, ci); kv_piece<1>(k, layer, ci); kv_piece<2>(k, layer, ci); }
#define EXP2(x) __builtin_amdgcn_exp2f(x)
#define GRP(Sx, i, pi) do { SBAR(); \
    const float e0_ = EXP2(Sx[i]), e1_ = EXP2(Sx[(i) + 1]), e2_ = EXP2(Sx[(i) + 2]), e3_ = EXP2(Sx[(i) + 3]); \
    P[pi] = pack2(e0_, e1_); P[(pi) + 1] = pack2(e2_, e3_); \
    asm volatile("" : "+v"(P[pi]), "+v"(P[(pi) + 1])); SBAR(); } while (0)

template <bool B> struct BoolTag { static constexpr bool value = B; };
template <int NFT> DI void attn_phase(const Ctx& k0, int layer, v4u (&wf)[15]) {
    const Ctx k = local_ctx(k0);
    const int hh = k.w & 3, kh = k.w >> 2, h = k.lane >> 5, l31 = k.lane & 31;
    LAS unsigned char* L = k.L;
    v4u qa0, qb0; v2u qa1, qb1;
    {
        const int o = L_QS + l31 * QS_STRIDE + 40 * hh;
        const v2u a0 = lds_ld<v2u>(L, o + 16 * h), a1 = lds_ld<v2u>(L, o + 16 * h + 8), a2 = lds_ld<v2u>(L, o + 32);
        const v2u b0 = lds_ld<v2u>(L, o + 32 * QS_STRIDE + 16 * h), b1 = lds_ld<v2u>(L, o + 32 * QS_STRIDE + 16 * h + 8), b2 = lds_ld<v2u>(L, o + 32 * QS_STRIDE + 32);
        qa0 = (v4u){a0.x, a0.y, a1.x, a1.y}; qb0 = (v4u){b0.x, b0.y, b1.x, b1.y};
        qa1 = h ? (v2u){0u, 0u} : a2; qb1 = h ? (v2u){0u, 0u} : b2;
    }
    float qn;
    {
        float na = 0.f, nb = 0.f;
#pragma unroll
        for (int m = 0; m < 4; ++m) { na += bflo(qa0[m]) * bflo(qa0[m]) + bfhi(qa0[m]) * bfhi(qa0[m]); nb += bflo(qb0[m]) * bflo(qb0[m]) + bfhi(qb0[m]) * bfhi(qb0[m]); }
#pragma unroll
        for (int m = 0; m < 2; ++m) { na += bflo(qa1[m]) * bflo(qa1[m]) + bfhi(qa1[m]) * bfhi(qa1[m]); nb += bflo(qb1[m]) * bflo(qb1[m]) + bfhi(qb1[m]) * bfhi(qb1[m]); }
        na = sum32(na); nb = sum32(nb);
        qn = fmaxf(na, nb);
        if (!(na == na && nb == nb)) qn = __builtin_inff();
        qn = wave_max(qn);
    }
    if (k.w == 0) {
        unsigned spins = SPIN_CAP;
        const gu32* word = k.ctrl + cnt_idx(CNT_KV, layer, k.lane & (NG - 1)) * 64;
#pragma unroll 1
        while (!__all(__hip_atomic_load(word, RLX_AGENT) >= 8u) && --spins) {}
        if (spins == 0 && k.lane == 0) __hip_atomic_store(k.ctrl + W_TMO, 1u, RLX_AGENT);
        __builtin_amdgcn_fence(__ATOMIC_ACQUIRE, "wavefront");
    }
    BAR();
    STAMP(6 + layer * 24);
    bool fast;
    {
        v2u kn, vn;
        const unsigned long long nb = gaddr(k, WS_KVN + layer * 1024);
        asm volatile("global_load_dwordx2 %0, %1, %2 sc1" : "=v"(kn) : "v"(k.lane * 8), "s"(nb) : "memory");
        asm volatile("global_load_dwordx2 %0, %1, %2 offset:512 sc1" : "=v"(vn) : "v"(k.lane * 8), "s"(nb) : "memory");
        kv_dma(k, layer, 0);
        asm volatile("s_waitcnt vmcnt(3)" : "+v"(kn), "+v"(vn)::"memory");
        const float km = wave_max(fmaxf(u2f(kn.x), u2f(kn.y))), vm = wave_max(fmaxf(u2f(vn.x), u2f(vn.y)));
        fast = (qn * km * 1.01f <= SAFE_BOUND * SAFE_BOUND) && (vm <= SAFE_VMAX);
    }
    wait_vm_bar<0>();
    STAMP(7 + layer * 24);
    kv_dma(k, layer, 1);

    f32x16 Oa, Ob;
#pragma unroll
    for (int i = 0; i < 16; ++i) { Oa[i] = 0.f; Ob[i] = 0.f; }
    float ma = 0.f, mb = 0.f;
    const int dd = l31 < 24 ? l31 : 22;
    int aK0 = L_ACT + kh * 12288 + k.lane16;
    int aK1 = L_ACT + kh * 12288 + 1024 + l31 * 8;
    int aV = L_ACT + kh * 12288 + 1280 + (h * 24 + dd) * 16;
    const f32x16 Z = {};
    if (fast) {
        f32x16 Sa, Sb;
        unsigned P[8];
        v4u K0 = lds_ld<v4u>(L, aK0), V0 = lds_ld<v4u>(L, aV), V1 = {0u, 0u, 0u, 0u};
        v2u K1 = lds_ld<v2u>(L, aK1);
        Sa = mfma32(K0, qa0, Z);
        Sa = mfma32k8(K1, qa1, Sa);
#pragma unroll
        for (int i = 0; i < 8; ++i) P[i] = 0u;
        auto chunk = [&](auto last_tag, int ci) {
            constexpr bool LASTC = decltype(last_tag)::value;
            const int nxt = (ci & 1) ? -KV_SLOT : KV_SLOT;
#define WLD(i) do { if (LASTC && (i) < 5 * NFT) { wf[i] = gemm80_w<NFT>(k, WS_WO, i); MEMFENCE(); } } while (0)
#pragma unroll
            for (int j = 0; j < 4; ++j) {
                Sb = mfma32(K0, qb0, Z);
                if (j < 3) K0 = lds_ld<v4u>(L, aK0 + (j + 1) * KV_TILE);
                GRP(Sa, 0, 0);
                Ob = mfma32(V1, (v4u){P[4], P[5], P[6], P[7]}, Ob);
                V1 = lds_ld<v4u>(L, aV + j * KV_TILE + 768);
                WLD(4 * j);
                GRP(Sa, 4, 2);
                if (j == 3) {
                    if (!LASTC) {
                        wait_vm_bar<0>();
                        if (ci < 6) kv_piece<0>(k, layer, ci + 2);
                    }
                    K0 = lds_ld<v4u>(L, aK0 + nxt);
                }
                Sb = mfma32k8(K1, qb1, Sb);
                K1 = j == 3 ? lds_ld<v2u>(L, aK1 + nxt) : lds_ld<v2u>(L, aK1 + (j + 1) * KV_TILE);
                GRP(Sa, 8, 4);
                Oa = mfma32(V0, (v4u){P[0], P[1], P[2], P[3]}, Oa);
                if (j == 3 && !LASTC && ci < 6) kv_piece<1>(k, layer, ci + 2);
                WLD(4 * j + 1);
                GRP(Sa, 12, 6);
                Sa = mfma32(K0, qa0, Z);
                GRP(Sb, 0, 0);
                Oa = mfma32(V1, (v4u){P[4], P[5], P[6], P[7]}, Oa);
                if (j == 3 && !LASTC && ci < 6) kv_piece<2>(k, layer, ci + 2);
                WLD(4 * j + 2);
                GRP(Sb, 4, 2);
                Sa = mfma32k8(K1, qa1, Sa);
                GRP(Sb, 8, 4);
                Ob = mfma32(V0, (v4u){P[0], P[1], P[2], P[3]}, Ob);
                V0 = j == 3 ? lds_ld<v4u>(L, aV + nxt) : lds_ld<v4u>(L, aV + (j + 1) * KV_TILE);
                WLD(4 * j + 3);
                GRP(Sb, 12, 6);
            }
#undef WLD
            aK0 += nxt; aK1 += nxt; aV += nxt;
        };
#pragma unroll 1
        for (int ci = 0; ci < 7; ++ci) chunk(BoolTag<false>{}, ci);
        chunk(BoolTag<true>{}, 7);
        SBAR();
        Ob = mfma32(V1, (v4u){P[4], P[5], P[6], P[7]}, Ob);
    } else {
        ma = -__builtin_inff(); mb = -__builtin_inff();
        for (int ci = 0; ci < 8; ++ci) {
            const int nxt = (ci & 1) ? -KV_SLOT : KV_SLOT;
            for (int j = 0; j < 4; ++j) {
                const v4u K0 = lds_ld<v4u>(L, aK0 + j * KV_TILE), V0 = lds_ld<v4u>(L, aV + j * KV_TILE), V1 = lds_ld<v4u>(L, aV + j * KV_TILE + 768);
                const v2u K1 = lds_ld<v2u>(L, aK1 + j * KV_TILE);
#pragma unroll
                for (int x = 0; x < 2; ++x) {
                    f32x16 Sx = mfma32(K0, x ? qb0 : qa0, Z);
                    Sx = mfma32k8(K1, x ? qb1 : qa1, Sx);
                    float mt = Sx[0];
#pragma unroll
                    for (int i = 1; i < 16; ++i) mt = fmaxf(mt, Sx[i]);
                    mt = max32(mt);
                    const float mo = x ? mb : ma, mn = fmaxf(mo, mt);
                    const float al = EXP2(mo - mn);
                    unsigned P[8];
#pragma unroll
                    for (int i = 0; i < 8; ++i) P[i] = pack2(EXP2(Sx[2 * i] - mn), EXP2(Sx[2 * i + 1] - mn));
                    f32x16 O = x ? Ob : Oa;
#pragma unroll
                    for (int i = 0; i < 16; ++i) O[i] *= al;
                    O = mfma32(V0, (v4u){P[0], P[1], P[2], P[3]}, O);
                    O = mfma32(V1, (v4u){P[4], P[5], P[6], P[7]}, O);
                    if (x) { Ob = O; mb = mn; } else { Oa = O; ma = mn; }
                }
            }
            if (ci < 7) {
                wait_vm_bar<0>();
                if (ci < 6) kv_dma(k, layer, ci + 2);
            }
            aK0 += nxt; aK1 += nxt; aV += nxt;
        }
        gemm80_load<NFT>(k, WS_WO, wf);
        wait_vm<0>();
    }
    STAMP(8 + layer * 24);
    if (k.w == 4) STAMPW(54 + layer);
    {
        const int xo = L_XO + k.w * 3328 + k.lane16;
#pragma unroll
        for (int i = 0; i < 3; ++i)
            lds_st<f32x4>(L, xo + i * 1024, kh ? (f32x4){Oa[4 * i], Oa[4 * i + 1], Oa[4 * i + 2], Oa[4 * i + 3]} : (f32x4){Ob[4 * i], Ob[4 * i + 1], Ob[4 * i + 2], Ob[4 * i + 3]});
        lds_st<float>(L, L_XO + k.w * 3328 + 3072 + k.lane * 4, kh ? ma : mb);
        BAR();
        const int xi = L_XO + (k.w ^ 4) * 3328;
        const float mo = lds_ld<float>(L, xi + 3072 + k.lane * 4), ms = kh ? mb : ma, mx = fmaxf(ms, mo);
        const float fs = EXP2(ms - mx), fo = EXP2(mo - mx);
        float o[12];
#pragma unroll
        for (int i = 0; i < 3; ++i) {
            const f32x4 t = lds_ld<f32x4>(L, xi + i * 1024 + k.lane16);
#pragma unroll
            for (int r = 0; r < 4; ++r) o[4 * i + r] = (kh ? Ob[4 * i + r] : Oa[4 * i + r]) * fs + t[r] * fo;
        }
        const auto sw = __builtin_amdgcn_permlane32_swap(f2u(o[8]), f2u(o[8]), false, false);
        const float lsum = u2f(h ? sw[1] : sw[1]);
        const float inv = __builtin_amdgcn_rcpf(lsum);
        const v4u pk = {pack2(o[0] * inv, o[1] * inv), pack2(o[2] * inv, o[3] * inv), pack2(o[4] * inv, o[5] * inv), pack2(o[6] * inv, o[7] * inv)};
        const int tok = 32 * kh + l31, T = tok >> 4, l15 = tok & 15;
        const int base = WS_OF + k.g * ACT_G;
        const int ch = 10 * k.c + 2 * hh + h;
        st128(k.rs, pk, base + (T * 20 + (ch >> 2)) * 1024 + ((ch & 3) * 16 + l15) * 16);
        if (h == 0) {
            const int c2 = 10 * k.c + 8 + (hh >> 1);
            const v2u p2 = {pack2(o[8] * inv, o[9] * inv), pack2(o[10] * inv, o[11] * inv)};
            __builtin_amdgcn_raw_buffer_store_b64(p2, k.rs, (T * 20 + (c2 >> 2)) * 1024 + ((c2 & 3) * 16 + l15) * 16 + (hh & 1) * 8, base, SC1);
        }
    }
    STAMP(9 + layer * 24);
    signal_cnt(k.ctrl, cnt_idx(CNT_O, layer, k.g), k.tid);
}

template <int LO, int HI> DI void ffn1_loadw(const Ctx& k, v4u (&w)[40]) {
    const int u = k.c + 8 * k.w;
    const unsigned vo = u < NU ? k.lane16 : 0x7fffff00u;
#pragma unroll
    for (int i = LO; i < HI; ++i) w[i] = ldw(k.rs, vo, WS_W1 + (u * 40 + (i & 1) * 20 + (i >> 1)) * 1024);
    MEMFENCE(); SBAR();
}
DI v4u ffn1_w(const Ctx& k, int i) {
    const int u = k.c + 8 * k.w;
    return ldw(k.rs, u < NU ? k.lane16 : 0x7fffff00u, WS_W1 + (u * 40 + (i & 1) * 20 + (i >> 1)) * 1024);
}
template <int NFT> DI void wo_body(const Ctx& k0, int layer, const v4u (&wf)[15], v4u (&wa)[40]) {
    const Ctx k = local_ctx(k0);
    wait_cnt(k.ctrl, cnt_idx(CNT_O, layer, k.g), 8u, k.tid);
    STAMP(11 + layer * 24);
    gemm80_dma(k, WS_OF);
    wait_vm_bar<5>();
    STAMP(12 + layer * 24);
    f32x4 acc[3];
    gemm80_mma<NFT, true>(k, wf, acc, [&](int n) {
        if (n >= 7 && n < 18 && (n - 7) % 3 != 2) { const int i = (n - 7) / 3 * 2 + (n - 7) % 3; wa[i] = ffn1_w(k, i); MEMFENCE(); } });
    STAMP(13 + layer * 24);
    BAR();
    f32x4 y[3];
    red_recv<NFT>(k, acc, y);
    resid_epilogue<NFT, false>(k, y, WS_X1F, WS_STX1, nullptr);
    signal_cnt(k.ctrl, cnt_idx(CNT_X1, layer, k.g), k.tid);
    ffn1_loadw<8, 20>(k, wa);
}

DI f32x2 mish2(f32x2 h) {
    const f32x2 e = h * LOG2E;
    const f32x2 u = {EXP2(fminf(e.x, 43.f)), EXP2(fminf(e.y, 43.f))};
    const f32x2 w = u * (u + 2.f), d = w + 2.f;
    const f32x2 r = {__builtin_amdgcn_rcpf(d.x), __builtin_amdgcn_rcpf(d.y)};
    return h * w * r;
}
template <int NFT> DI void ffn2_loadw(const Ctx& k, int ch, v4u (&wf)[2 * NFT]) {
    const int kq = k.w & 3, ft0 = NFT == 3 ? 0 : 3;
    int u0 = 8 * ch + 2 * kq;
    if (ch == 6 && kq == 3) u0 = NU - 2;
#pragma unroll
    for (int s = 0; s < 2; ++s)
#pragma unroll
        for (int a = 0; a < NFT; ++a) wf[s * NFT + a] = ldw(k.rs, k.lane16, WS_W2 + ((k.c * 5 + ft0 + a) * NU + u0 + s) * 1024);
    MEMFENCE(); SBAR();
}
template <int NFT> DI v4u ffn2_w(const Ctx& k, int ch, int j) {
    const int kq = k.w & 3;
    const int u0 = (ch == 6 && kq == 3) ? NU - 2 : 8 * ch + 2 * kq;
    return ldw(k.rs, k.lane16, WS_W2 + ((k.c * 5 + (NFT == 3 ? 0 : 3) + j % NFT) * NU + u0 + j / NFT) * 1024);
}
template <int NFT> struct W2 { v4u c[6][2 * NFT]; };
template <int NFT> DI void w2_load(const Ctx& k, W2<NFT>& w2, int i) {
    if (i < 12 * NFT) { w2.c[i / (2 * NFT)][i % (2 * NFT)] = ffn2_w<NFT>(k, i / (2 * NFT), i % (2 * NFT)); MEMFENCE(); }
}
template <int NFT> DI void ffn1_phase(const Ctx& k0, int layer, const u16* b1, v4u (&wa)[40], W2<NFT>& w2) {
    const Ctx k = local_ctx(k0);
    const int u = k.c + 8 * k.w;
    const bool active = u < NU;
    const int q4 = k.lane >> 4, l15 = k.lane & 15;
    f32x4 cs[2]; v2u bb[2];
    {
        const int uc = active ? u : 0;
#pragma unroll
        for (int a = 0; a < 2; ++a) {
            cs[a] = *(const GAS f32x4*)(k.ws + WS_CS1 + (32 * uc + 16 * a + 4 * q4) * 4);
            bb[a] = *(const GAS v2u*)(b1 + 32 * uc + 16 * a + 4 * q4);
        }
    }
    wait_cnt(k.ctrl, cnt_idx(CNT_X1, layer, k.g), 8u, k.tid);
    STAMP(15 + layer * 24);
    Stats st;
    if (k.w == 7) stats_issue(k, WS_STX1, st);
    {
        const int gb = WS_X1F + k.g * ACT_G;
        const int oa = ((k.w >> 1) * 20 + 2 * (k.w & 1)) * 1024, ob = ((k.w >> 1) * 20 + 4 + 8 * (k.w & 1)) * 1024;
        dma<0>(k.lane16, gaddr(k, gb + oa), L_ACT + oa); dma<1024>(k.lane16, gaddr(k, gb + oa), L_ACT + oa);
        QBAR();
        const unsigned long long sb = gaddr(k, gb + ob);
        dma<0>(k.lane16, sb, L_ACT + ob); dma<1024>(k.lane16, sb, L_ACT + ob); dma<2048>(k.lane16, sb, L_ACT + ob); dma<3072>(k.lane16, sb, L_ACT + ob);
        dma<0>(k.lane16, sb + 4096, L_ACT + ob + 4096); dma<1024>(k.lane16, sb + 4096, L_ACT + ob + 4096);
        dma<2048>(k.lane16, sb + 4096, L_ACT + ob + 4096); dma<3072>(k.lane16, sb + 4096, L_ACT + ob + 4096);
    }
    wait_vm_bar<8>();
    STAMP(16 + layer * 24);
    if (active) {
        f32x4 acc[2][4];
#pragma unroll
        for (int a = 0; a < 2; ++a)
#pragma unroll
            for (int T = 0; T < 4; ++T) acc[a][T] = (f32x4){0.f, 0.f, 0.f, 0.f};
        const int ab = L_ACT + k.lane16;
        constexpr int RB = 6;
        v4u b[RB];
#pragma unroll
        for (int n = 0; n < RB - 1; ++n) b[n] = lds_ld<v4u>(k.L, ab + ((n & 3) * 20 + (n >> 2)) * 1024);
#pragma unroll
        for (int n = 0; n < 80; ++n) {
            if (n == 17 - RB) {
                wait_vm_bar<(17 - RB + 1) / 2>();
                if (k.w < 4) __builtin_amdgcn_s_setprio(1);
            }
            if (!(n & 1) && n < 40) { wa[20 + (n >> 1)] = ffn1_w(k, 20 + (n >> 1)); MEMFENCE(); }
            if (n >= 42 && n % 3 == 0) w2_load<NFT>(k, w2, (n - 42) / 3);
            if (n + RB - 1 < 80) b[(n + RB - 1) % RB] = lds_ld<v4u>(k.L, ab + (((n + RB - 1) & 3) * 20 + ((n + RB - 1) >> 2)) * 1024);
            SBAR();
            const int ks = n >> 2;
            acc[0][n & 3] = mfma16(wa[2 * ks], b[n % RB], acc[0][n & 3]);
            acc[1][n & 3] = mfma16(wa[2 * ks + 1], b[n % RB], acc[1][n & 3]);
            SBAR();
        }
        __builtin_amdgcn_s_setprio(0);
        STAMP(17 + layer * 24);
        if (k.w == 4) STAMPW(48 + layer);
        f32x2 bi[2][2];
#pragma unroll
        for (int a = 0; a < 2; ++a)
#pragma unroll
            for (int r = 0; r < 2; ++r) bi[a][r] = (f32x2){bflo(bb[a][r]), bfhi(bb[a][r])};
#pragma unroll
        for (int T = 0; T < 4; ++T) {
            const f32x2 sv = lds_ld<f32x2>(k.L, L_STAT + (T * 16 + l15) * 8);
            v4u o;
#pragma unroll
            for (int a = 0; a < 2; ++a) {
                if (2 * T + a < 8) { w2_load<NFT>(k, w2, 13 + 2 * T + a); if (2 * T + a < 3) w2_load<NFT>(k, w2, 21 + 2 * T + a); }
#pragma unroll
                for (int r = 0; r < 2; ++r) {
                    const f32x2 x = {acc[a][T][2 * r], acc[a][T][2 * r + 1]}, c2 = {cs[a][2 * r], cs[a][2 * r + 1]};
                    const f32x2 m = mish2((x - c2 * sv.x) * sv.y + bi[a][r]);
                    o[a * 2 + r] = pack2(m.x, m.y);
                }
                SBAR();
            }
            st128(k.rs, o, k.lane16 + WS_HF + k.g * HF_G + (T * NU + u) * 1024);
            SBAR();
        }
        if (k.w == 0) STAMPW(50 + layer);
        if (k.w == 4) STAMPW(52 + layer);
    } else {
        wait_vm<0>();
        if (k.w == 7) stats_finish(k, st);
        BAR();
#pragma unroll
        for (int i = 0; i < 24; ++i) w2_load<NFT>(k, w2, i);
        wait_vm<0>();
    }
    signal_cnt(k.ctrl, cnt_idx(CNT_H, layer, k.g), k.tid);
#pragma unroll
    for (int i = 24; i < 36; ++i) w2_load<NFT>(k, w2, i);
    SBAR();
}

DI void ffn2_piece(const Ctx& k, int layer, int ch, int T) {
    if (ch < 6 || k.w < 6)
        dma<0>(k.lane16, gaddr(k, WS_HF + k.g * HF_G + (T * NU + 8 * ch + k.w) * 1024), L_ACT + (ch & 3) * 32768 + k.w * 4096 + T * 1024);
}
template <int NFT, int CH, class Hook> DI void ffn2_chunk(const Ctx& k, const v4u (&wf)[2 * NFT], f32x4 (&acc)[3][4], v4u (&b)[4], const int (&ab)[4], Hook hook) {
    const int kq = k.w & 3;
    const bool live = CH < 6 || kq < 3, livenext = CH + 1 < 6 || kq < 3;
#pragma unroll
    for (int m = 0; m < 8; ++m) {
        hook(8 * CH + m);
        const int n = m + 3;
        if (n < 8) { if (live) b[n & 3] = lds_ld<v4u>(k.L, ab[n & 3] + (CH & 3) * 32768 + (n >> 2) * 4096); }
        else if (CH < 6) { if (livenext) b[n & 3] = lds_ld<v4u>(k.L, ab[n & 3] + ((CH + 1) & 3) * 32768 + ((n - 8) >> 2) * 4096); }
        SBAR();
        if (live) {
#pragma unroll
            for (int a = 0; a < NFT; ++a) acc[a][m & 3] = mfma16(wf[(m >> 2) * NFT + a], b[m & 3], acc[a][m & 3]);
        }
        SBAR();
    }
}
template <int NFT> DI void ffn2_body(const Ctx& k0, int layer, u16* out, W2<NFT>& w2) {
    const Ctx k = local_ctx(k0);
    const int kq = k.w & 3;
    wait_cnt(k.ctrl, cnt_idx(CNT_H, layer, k.g), 8u, k.tid);
    STAMP(19 + layer * 24);
#pragma unroll
    for (int i = 0; i < 12; ++i) { ffn2_piece(k, layer, i >> 2, i & 3); if (i == 3 || i == 7) QBAR(); }
    f32x4 acc[3][4];
#pragma unroll
    for (int a = 0; a < 3; ++a)
#pragma unroll
        for (int T = 0; T < 4; ++T) acc[a][T] = (f32x4){0.f, 0.f, 0.f, 0.f};
    int ab[4];
#pragma unroll
    for (int dT = 0; dT < 4; ++dT) ab[dT] = L_ACT + 2 * kq * 4096 + ((kq + dT) & 3) * 1024 + k.lane16;
    constexpr int NW = 2 * NFT;
    wait_vm_bar<8>();
    STAMP(20 + layer * 24);
    v4u b[4], w6[NW];
#pragma unroll
    for (int n = 0; n < 3; ++n) b[n] = lds_ld<v4u>(k.L, ab[n]);
    auto hook = [&](int t) {
        if (t == 5) wait_vm_bar<4>();
        if (t == 13) wait_vm_bar<4 + 3>();
        if (t == 21) wait_vm_bar<4 + (NW - 2)>();
        if (t == 29) wait_vm_bar<4>();
        if (t == 37) { if (k.w < 6) wait_vm_bar<4>(); else wait_vm_bar<0>(); }
        if (t == 45) wait_vm_bar<0>();
        if (t >= 5 && t < 37 && (t & 1)) ffn2_piece(k, layer, 3 + (t - 5) / 8, ((t - 5) >> 1) & 3);
        if (t >= 8 && t < 8 + 2 * NW && !(t & 1)) { w6[(t - 8) >> 1] = ffn2_w<NFT>(k, 6, (t - 8) >> 1); MEMFENCE(); }
    };
    ffn2_chunk<NFT, 0>(k, w2.c[0], acc, b, ab, hook);
    ffn2_chunk<NFT, 1>(k, w2.c[1], acc, b, ab, hook);
    ffn2_chunk<NFT, 2>(k, w2.c[2], acc, b, ab, hook);
    ffn2_chunk<NFT, 3>(k, w2.c[3], acc, b, ab, hook);
    ffn2_chunk<NFT, 4>(k, w2.c[4], acc, b, ab, hook);
    ffn2_chunk<NFT, 5>(k, w2.c[5], acc, b, ab, hook);
    ffn2_chunk<NFT, 6>(k, w6, acc, b, ab, hook);
    BAR();
    STAMP(21 + layer * 24);
    red_send<NFT>(k, acc);
    BAR();
    STAMP(22 + layer * 24);
    f32x4 y[3];
    { const f32x4 own[3] = {acc[0][0], acc[1][0], acc[2][0]}; red_recv<NFT>(k, own, y); }
    if (layer == 0) {
        resid_epilogue<NFT, false>(k, y, WS_XF, WS_STX, out);
        signal_cnt(k.ctrl, cnt_idx(CNT_X2, 0, k.g), k.tid);
    } else {
        resid_epilogue<NFT, true>(k, y, WS_XF, WS_STX, out);
        wait_vm<0>();
    }
}

template <int NFT> DI void layers(const Ctx& k, const u16* b1, u16* out) {
    qkv_body<NFT, 0>(k);
    STAMP(5);
#pragma unroll 1
    for (int layer = 0; layer < 2; ++layer) {
        v4u wf[15];
        attn_phase<NFT>(k, layer, wf);
        STAMP(10 + layer * 24);
        v4u wa[40];
        wo_body<NFT>(k, layer, wf, wa);
        STAMP(14 + layer * 24);
        W2<NFT> w2;
        ffn1_phase<NFT>(k, layer, b1, wa, w2);
        STAMP(18 + layer * 24);
        ffn2_body<NFT>(k, layer, out, w2);
        STAMP(23 + layer * 24);
        if (layer == 0) {
            qkv_body<NFT, 1>(k);
            STAMP(29);
        }
    }
}

extern "C" __global__ __launch_bounds__(512, 2) void main_kernel(const u16* __restrict__ b1, u16* __restrict__ out, unsigned char* __restrict__ ws) {
    __shared__ __attribute__((aligned(16))) unsigned char smem[LDS_BYTES];
    Ctx k;
    k.ws = ws;
    k.rs = __builtin_amdgcn_make_buffer_rsrc((void*)ws, 0, WS_END, 0x00020000);
    k.L = (LAS unsigned char*)smem;
    k.ctrl = (gu32*)(ws + WS_CTRL);
    k.tid = threadIdx.x; k.lane = threadIdx.x & 63; k.w = __builtin_amdgcn_readfirstlane(threadIdx.x >> 6);
    k.lane16 = k.lane * 16;
    const int b = blockIdx.x, i = b >> 3;
    k.g = (b & 7) * 4 + (i >> 3); k.c = i & 7;
    STAMP(0);
    {
        unsigned lo0, hi0, lo1, hi1;
        asm volatile("s_getpc_b64 s[90:91]\n\ts_mov_b32 %0, s90\n\ts_mov_b32 %1, s91\n\t"
                     "s_getpc_b64 s[90:91]\n\ts_add_u32 s90, s90, end_marker_kernel@rel32@lo+4\n\ts_addc_u32 s91, s91, end_marker_kernel@rel32@hi+12\n\t"
                     "s_mov_b32 %2, s90\n\ts_mov_b32 %3, s91"
                     : "=s"(lo0), "=s"(hi0), "=s"(lo1), "=s"(hi1) :: "s90", "s91", "scc");
        const unsigned long long c0 = (((unsigned long long)hi0 << 32) | lo0) & ~255ull, c1 = ((unsigned long long)hi1 << 32) | lo1;
        const unsigned off = ((unsigned)(i * 8 + k.w) * 32u + (k.lane & 31)) * 16u;
        if (k.lane < 32 && c0 + off + 16 <= c1)
            asm volatile("s_mov_b32 m0, %2\n\ts_nop 0\n\tglobal_load_lds_dwordx4 %0, %1" ::"v"(off), "s"(c0), "s"(L_RED + k.w * 1024) : "memory");
    }
    if (k.w >> 2) layers<2>(k, b1, out); else layers<3>(k, b1, out);
}
extern "C" __global__ void end_marker_kernel() {}

extern "C" void kernel_launch(void* const* d_in, const int* in_sizes, int n_in, void* d_out, int out_size, void* d_ws, size_t ws_size,
                              hipStream_t stream) {
    (void)in_sizes; (void)n_in; (void)out_size; (void)ws_size;
    const u16* x = (const u16*)d_in[0];
    const u16* wq = (const u16*)d_in[1];
    const u16* wk = (const u16*)d_in[2];
    const u16* wv = (const u16*)d_in[3];
    const u16* wo = (const u16*)d_in[4];
    const u16* w1 = (const u16*)d_in[6];
    const u16* b1 = (const u16*)d_in[7];
    const u16* w2 = (const u16*)d_in[8];
    unsigned char* ws = (unsigned char*)d_ws;
    prep_kernel<<<PB_TOTAL, 256, 0, stream>>>(x, wq, wk, wv, wo, w1, w2, ws);
    main_kernel<<<256, 512, 0, stream>>>(b1, (u16*)d_out, ws);
}
```
